```python
import torch

def _softmax(x, axis=-1):
    return torch.softmax(x, dim=axis)
def _gelu(x):
    return torch.nn.functional.gelu(x, approximate="tanh")
def _silu(x):
    return torch.nn.functional.silu(x)
def _sigmoid(x):
    return torch.sigmoid(x)
def _layernorm(x, axis=-1, eps=1e-5):
    m = x.mean(dim=axis, keepdim=True)
    v = x.var(dim=axis, keepdim=True, unbiased=False)
    return (x - m) / torch.sqrt(v + eps)
def _scatter_add(tgt, idx, src, axis=0):
    return tgt.clone().index_add_(axis, idx, src)
def _segment_sum(src, seg, n):
    out = torch.zeros((n,) + src.shape[1:], dtype=src.dtype)
    return out.index_add_(0, seg, src)
def _pad(x, pw):
    flat = tuple(v for pair in reversed(pw) for v in pair)
    return torch.nn.functional.pad(x, flat)

kernel_name = 'standalone__mha_block_cached__sampling'

def setup_inputs():

    _w = (lambda a: (lambda t: t.to(torch.bfloat16) if t.is_floating_point() else t)(torch.as_tensor(a)))
    B, Sq, D = 16, 1, 1024
    H, Sk, Dh = 32, 2048, 128
    return {
        "x": _w((lambda *s: torch.randn(*s, dtype=torch.bfloat16) * 0.8)(B, Sq, D)),
        "k_cache": _w((lambda *s: torch.randn(*s, dtype=torch.bfloat16) * 0.8)(B, H, Sk, Dh)),
        "v_cache": _w((lambda *s: torch.randn(*s, dtype=torch.bfloat16) * 0.05)(B, H, Sk, Dh)),
        "wq": _w((lambda *s: torch.randn(*s, dtype=torch.bfloat16) * 0.8)(D, H * Dh) * (D ** -0.5)),
        "wo": _w((lambda *s: torch.randn(*s, dtype=torch.bfloat16) * 0.05)(H * Dh, D) * ((H * Dh) ** -0.5)),
    }

def reference(x, k_cache, v_cache, wq, wo):
    B, Sq, D = x.shape
    H, Dh = 32, 128
    q = (lambda x, *d: x.permute(*d))((x @ wq).reshape(B, Sq, H, Dh), 0, 2, 1, 3)
    scores = torch.einsum("bhqd,bhkd->bhqk", q, k_cache) * (Dh ** -0.5)
    p = _softmax(scores, axis=-1)
    o = torch.einsum("bhqk,bhkd->bhqd", p, v_cache)
    return (lambda x, *d: x.permute(*d))(o, 0, 2, 1, 3).reshape(B, Sq, H * Dh) @ wo
```

```cpp
#include <hip/hip_runtime.h>
#include <hip/hip_bf16.h>
#include <stdint.h>

typedef unsigned u32x4 __attribute__((ext_vector_type(4)));
typedef float f32x4 __attribute__((ext_vector_type(4)));
typedef __attribute__((ext_vector_type(2))) __bf16 bf16x2;

constexpr int B = 16, D = 1024, H = 32, SK = 2048, DH = 128, HD = H * DH;

__device__ __forceinline__ float dot2(unsigned k, unsigned q, float acc) {
  return __builtin_amdgcn_fdot2_f32_bf16(__builtin_bit_cast(bf16x2, k), __builtin_bit_cast(bf16x2, q), acc, false);
}
__device__ __forceinline__ float bflo(unsigned u) { return __builtin_bit_cast(float, u << 16); }
__device__ __forceinline__ float bfhi(unsigned u) { return __builtin_bit_cast(float, u & 0xffff0000u); }
__device__ __forceinline__ unsigned short f2bf(float f) {
  __bf16 b = (__bf16)f; return __builtin_bit_cast(unsigned short, b);
}

template <int K, int N>
__global__ void __launch_bounds__(256) gemm16(const unsigned short* __restrict__ x, const unsigned short* __restrict__ w, unsigned short* __restrict__ y) {
  __shared__ float red[128][16][17];
  const int t = threadIdx.x, kk = t >> 1, half = t & 1;
  const int n0 = blockIdx.x * 16 + half * 8;
  float acc[16][8];
#pragma unroll
  for (int b = 0; b < 16; ++b)
#pragma unroll
    for (int j = 0; j < 8; ++j) acc[b][j] = 0.f;
  for (int k = kk; k < K; k += 128) {
    u32x4 wv = *reinterpret_cast<const u32x4*>(w + (size_t)k * N + n0);
    float wf[8];
#pragma unroll
    for (int j = 0; j < 4; ++j) { wf[2 * j] = bflo(wv[j]); wf[2 * j + 1] = bfhi(wv[j]); }
#pragma unroll
    for (int b = 0; b < 16; ++b) {
      float xv = __builtin_bit_cast(float, (unsigned)x[b * K + k] << 16);
#pragma unroll
      for (int j = 0; j < 8; ++j) acc[b][j] = fmaf(xv, wf[j], acc[b][j]);
    }
  }
#pragma unroll
  for (int b = 0; b < 16; ++b)
#pragma unroll
    for (int j = 0; j < 8; ++j) red[kk][b][half * 8 + j] = acc[b][j];
  __syncthreads();
  {
    const int b = t >> 4, c = t & 15;
    float s = 0.f;
    for (int i = 0; i < 128; ++i) s += red[i][b][c];
    y[b * N + blockIdx.x * 16 + c] = f2bf(s);
  }
}

template <int CTRL> __device__ __forceinline__ float dpp(float x) {
  return __builtin_bit_cast(float, __builtin_amdgcn_mov_dpp(__builtin_bit_cast(int, x), CTRL, 0xf, 0xf, true));
}
constexpr int XOR1 = 0xB1, XOR2 = 0x4E, XOR7 = 0x141, XOR8 = 0x128, ROR4 = 0x124;

__global__ void __launch_bounds__(256) attn(const unsigned short* __restrict__ q, const unsigned short* __restrict__ kc, const unsigned short* __restrict__ vc, unsigned short* __restrict__ o) {
  __shared__ float sm[4][4][132];
  const int bh = blockIdx.x;
  const int b = bh / H, h = bh % H;
  const int tid = threadIdx.x, lane = tid & 63, wave = tid >> 6, r = lane >> 4, c = lane & 15;
  const u32x4 qv = *reinterpret_cast<const u32x4*>(q + b * HD + h * DH + 8 * c);
  const unsigned short* kb = kc + (size_t)bh * SK * DH + (size_t)(wave * 16 + r) * DH + 8 * c;
  const unsigned short* vb = vc + (size_t)bh * SK * DH + (size_t)(wave * 16 + r) * DH + 8 * c;
  constexpr float C2 = 0.088388347648318440f * 1.4426950408889634f;
  float m = -INFINITY, l = 0.f, acc[8];
#pragma unroll
  for (int j = 0; j < 8; ++j) acc[j] = 0.f;
  u32x4 Kn[4], Vn[4];
#pragma unroll
  for (int t = 0; t < 4; ++t) { Kn[t] = __builtin_nontemporal_load(reinterpret_cast<const u32x4*>(kb + t * 4 * DH)); Vn[t] = __builtin_nontemporal_load(reinterpret_cast<const u32x4*>(vb + t * 4 * DH)); }
  for (int i = 0; i < SK / 64; ++i) {
    u32x4 Kc[4], Vc[4];
#pragma unroll
    for (int t = 0; t < 4; ++t) { Kc[t] = Kn[t]; Vc[t] = Vn[t]; }
    if (i + 1 < SK / 64) {
#pragma unroll
      for (int t = 0; t < 4; ++t) { Kn[t] = __builtin_nontemporal_load(reinterpret_cast<const u32x4*>(kb + (size_t)(i + 1) * 64 * DH + t * 4 * DH)); Vn[t] = __builtin_nontemporal_load(reinterpret_cast<const u32x4*>(vb + (size_t)(i + 1) * 64 * DH + t * 4 * DH)); }
    }
    float s[4];
#pragma unroll
    for (int t = 0; t < 4; ++t) {
      float d = 0.f;
#pragma unroll
      for (int k = 0; k < 4; ++k) d = dot2(Kc[t][k], qv[k], d);
      d += dpp<XOR1>(d); d += dpp<XOR2>(d); d += dpp<XOR7>(d); d += dpp<XOR8>(d);
      s[t] = d * C2;
    }
    float pm = fmaxf(fmaxf(s[0], s[1]), fmaxf(s[2], s[3]));
    float mn = fmaxf(m, pm);
    float alpha = __builtin_amdgcn_exp2f(m - mn);
    m = mn;
    float p[4];
#pragma unroll
    for (int t = 0; t < 4; ++t) p[t] = __builtin_amdgcn_exp2f(s[t] - mn);
    l = l * alpha + (p[0] + p[1]) + (p[2] + p[3]);
#pragma unroll
    for (int j = 0; j < 8; ++j) acc[j] *= alpha;
#pragma unroll
    for (int t = 0; t < 4; ++t)
#pragma unroll
      for (int k = 0; k < 4; ++k) { acc[2 * k] = fmaf(p[t], bflo(Vc[t][k]), acc[2 * k]); acc[2 * k + 1] = fmaf(p[t], bfhi(Vc[t][k]), acc[2 * k + 1]); }
  }
#pragma unroll
  for (int j = 0; j < 8; ++j) sm[wave][r][8 * c + j] = acc[j];
  if (c == 0) { sm[wave][r][128] = m; sm[wave][r][129] = l; }
  __syncthreads();
  if (tid < 128) {
    float M = -INFINITY;
    for (int w = 0; w < 16; ++w) M = fmaxf(M, sm[w >> 2][w & 3][128]);
    float L = 0.f, O = 0.f;
    for (int w = 0; w < 16; ++w) { float f = __builtin_amdgcn_exp2f(sm[w >> 2][w & 3][128] - M); L += f * sm[w >> 2][w & 3][129]; O += f * sm[w >> 2][w & 3][tid]; }
    o[b * HD + h * DH + tid] = f2bf(O / L);
  }
}

extern "C" void kernel_launch(void* const* d_in, const int* in_sizes, int n_in, void* d_out, int out_size, void* d_ws, size_t ws_size, hipStream_t stream) {
  const unsigned short* x = (const unsigned short*)d_in[0];
  const unsigned short* kc = (const unsigned short*)d_in[1];
  const unsigned short* vc = (const unsigned short*)d_in[2];
  const unsigned short* wq = (const unsigned short*)d_in[3];
  const unsigned short* wo = (const unsigned short*)d_in[4];
  unsigned short* q = (unsigned short*)d_ws;
  unsigned short* o = q + B * HD;
  gemm16<D, HD><<<HD / 16, 256, 0, stream>>>(x, wq, q);
  attn<<<B * H, 256, 0, stream>>>(q, kc, vc, o);
  gemm16<HD, D><<<D / 16, 256, 0, stream>>>(o, wo, (unsigned short*)d_out);
}
```

```cpp
#include <hip/hip_runtime.h>
#include <stdint.h>

typedef unsigned u32x4 __attribute__((ext_vector_type(4)));
typedef float f32x4 __attribute__((ext_vector_type(4)));
typedef __attribute__((ext_vector_type(2))) __bf16 bf16x2;
typedef __attribute__((ext_vector_type(8))) __bf16 bf16x8;
typedef unsigned short u16;
typedef unsigned long long u64;

constexpr int B = 16, D = 1024, H = 32, SK = 2048, DH = 128, HD = H * DH;
#define WQ_AUX 2
#define WO_AUX 2
#define WO_MODE 1
#define USP0 2
#define USP1 3
#define STAG 2

constexpr unsigned WS_EPOCH = 0;
constexpr unsigned WS_FLAGS = 4096;
constexpr unsigned WS_BOX = 8192;
constexpr unsigned WS_BYTES = WS_BOX + 256 * 8192;
constexpr unsigned SENT = 0xffffffffu;
constexpr int SC1 = 16;

__device__ __forceinline__ float dot2(unsigned k, unsigned q, float acc) {
  return __builtin_amdgcn_fdot2_f32_bf16(__builtin_bit_cast(bf16x2, k), __builtin_bit_cast(bf16x2, q), acc, false);
}
__device__ __forceinline__ float asf(unsigned u) { return __builtin_bit_cast(float, u); }
__device__ __forceinline__ unsigned asu(float f) { return __builtin_bit_cast(unsigned, f); }
__device__ __forceinline__ float bflo(unsigned u) { return asf(u << 16); }
__device__ __forceinline__ float bfhi(unsigned u) { return asf(u & 0xffff0000u); }
__device__ __forceinline__ u16 f2bf(float f) { __bf16 b = (__bf16)f; return __builtin_bit_cast(u16, b); }
__device__ __forceinline__ unsigned pk2(float lo, float hi) { bf16x2 v = {(__bf16)lo, (__bf16)hi}; return __builtin_bit_cast(unsigned, v); }
template <int CTRL> __device__ __forceinline__ float dpp(float x) {
  return __builtin_bit_cast(float, __builtin_amdgcn_mov_dpp(__builtin_bit_cast(int, x), CTRL, 0xf, 0xf, true));
}
__device__ __forceinline__ float fold32(float a, float b) { auto r = __builtin_amdgcn_permlane32_swap(asu(a), asu(b), false, false); return asf(r[0]) + asf(r[1]); }
__device__ __forceinline__ float fold16(float a, float b) { auto r = __builtin_amdgcn_permlane16_swap(asu(a), asu(b), false, false); return asf(r[0]) + asf(r[1]); }
__device__ __forceinline__ float rowmax(float x) {
  auto r = __builtin_amdgcn_permlane16_swap(asu(x), asu(x), false, false); x = fmaxf(asf(r[0]), asf(r[1]));
  auto s = __builtin_amdgcn_permlane32_swap(asu(x), asu(x), false, false); return fmaxf(asf(s[0]), asf(s[1]));
}
constexpr int XOR1 = 0xB1, XOR2 = 0x4E, XOR7 = 0x141, XOR8 = 0x128;
#define SB() __builtin_amdgcn_sched_barrier(0)
#define BAR() asm volatile("s_waitcnt lgkmcnt(0)\n\ts_barrier" ::: "memory")

#define STAMP(k) do {} while (0)

struct Smem {
  float qpart[8][2][128];
  float pacc[2][8][128];
  float pm[2][8], pl[2][8];
  u16 o[2][128];
};

__device__ __forceinline__ f32x4 diag_proj(const u32x4 (&W)[32], const u32x4 (&xr)[4], int c) {
  unsigned sel[2][4];
#pragma unroll
  for (int par = 0; par < 2; ++par)
#pragma unroll
    for (int r = 0; r < 4; ++r) {
      const unsigned src = par ? 0x0302u : 0x0100u;
      const unsigned v = (c & 1) ? ((src << 16) | 0x0c0cu) : (0x0c0c0000u | src);
      sel[par][r] = (r == ((c & 7) >> 1)) ? v : 0x0c0c0c0cu;
    }
  f32x4 acc0 = {0.f, 0.f, 0.f, 0.f}, acc1 = {0.f, 0.f, 0.f, 0.f};
#pragma unroll
  for (int i = 0; i < 32; ++i) {
    const unsigned xv = xr[i >> 3][(i >> 1) & 3];
    u32x4 bo;
#pragma unroll
    for (int r = 0; r < 4; ++r) bo[r] = __builtin_amdgcn_perm(xv, xv, sel[i & 1][r]);
    if (i & 1) acc1 = __builtin_amdgcn_mfma_f32_16x16x32_bf16(__builtin_bit_cast(bf16x8, W[i]), __builtin_bit_cast(bf16x8, bo), acc1, 0, 0, 0);
    else acc0 = __builtin_amdgcn_mfma_f32_16x16x32_bf16(__builtin_bit_cast(bf16x8, W[i]), __builtin_bit_cast(bf16x8, bo), acc0, 0, 0, 0);
    SB();
  }
  return acc0 + acc1;
}

__global__ void __launch_bounds__(512, 2) mha_fused(const u16* x, const u16* kc, const u16* vc, const u16* wq, const u16* wo, u16* out, char* ws, u64* stamps) {
  __shared__ __attribute__((aligned(16))) Smem sm;
  const int tid = threadIdx.x, lane = tid & 63;
  const int wave = __builtin_amdgcn_readfirstlane(tid >> 6);
  const int xcd = blockIdx.x & 7, j = blockIdx.x >> 3;
  const int h = 4 * xcd + (j & 3), bp = j >> 2, b0 = 2 * bp;
  const int g = lane >> 4, c = lane & 15;
  if (wave == 0) STAMP(0);
  auto rk = __builtin_amdgcn_make_buffer_rsrc((void*)kc, 0, B * H * SK * DH * 2, 0x00020000);
  auto rv = __builtin_amdgcn_make_buffer_rsrc((void*)vc, 0, B * H * SK * DH * 2, 0x00020000);
  auto rs = __builtin_amdgcn_make_buffer_rsrc((void*)ws, 0, WS_BYTES, 0x00020000);
  const int voff = lane * 16;
  constexpr unsigned ITEM_STRIDE = H * SK * DH * 2;
  constexpr int NU = 16;
  const unsigned hsh = (blockIdx.x * 0x9E3779B1u) >> 20;
  const int rot = STAG >= 2 ? (hsh & (NU - 1)) : 0;
  const unsigned base0 = (unsigned)(b0 * H + h) * (SK * DH * 2) + wave * 4096;
#define UOFF(it, u) (base0 + (it) * ITEM_STRIDE + (((u) + rot) & (NU - 1)) * 32768)
  u32x4 K0[4], V0[4], K1[4], V1[4];
#define LDK(R, vo, so) _Pragma("unroll") for (int t_ = 0; t_ < 4; ++t_) { R[t_] = __builtin_amdgcn_raw_buffer_load_b128(rk, (vo) + t_ * 1024, (so), 2); asm volatile("" ::: "memory"); }
#define LDV(R, vo, so) _Pragma("unroll") for (int t_ = 0; t_ < 4; ++t_) { R[t_] = __builtin_amdgcn_raw_buffer_load_b128(rv, (vo) + t_ * 1024, (so), 2); asm volatile("" ::: "memory"); }
  const int boxo = WS_BOX + (bp * 32 + g) * 8192 + h * 256 + c * 16;
  u64 tag = 0;
  if (wave == 0) {
    tag = __hip_atomic_load(reinterpret_cast<u64*>(ws + WS_EPOCH), __ATOMIC_RELAXED, __HIP_MEMORY_SCOPE_AGENT);
    const u32x4 sv = {SENT, SENT, SENT, SENT};
#pragma unroll
    for (int w = 0; w < 8; ++w) __builtin_amdgcn_raw_buffer_store_b128(sv, rs, boxo, w * 4 * 8192, SC1);
    asm volatile("" ::: "memory");
  }
  SB();
  u32x4 W[32];
  {
    const int blk = (wave + bp) & 7;
    const int krow0 = 128 * blk + 32 * g;
    auto rw = __builtin_amdgcn_make_buffer_rsrc((void*)wq, 0, D * HD * 2, 0x00020000);
    const int wvo = krow0 * (HD * 2) + h * (DH * 2) + c * 16;
    u32x4 xr[4];
    const u16* xp = x + (b0 + (c >> 3)) * D + krow0;
#pragma unroll
    for (int i = 0; i < 4; ++i) xr[i] = *reinterpret_cast<const u32x4*>(xp + 8 * i);
    SB();
#pragma unroll
    for (int i = 0; i < 32; ++i) { W[i] = __builtin_amdgcn_raw_buffer_load_b128(rw, wvo, i * (HD * 2), WQ_AUX); asm volatile("" ::: "memory"); }
    SB();
    LDK(K0, voff, UOFF(0, 0)); SB(); LDV(V0, voff, UOFF(0, 0)); SB();
    LDK(K1, voff, UOFF(1, 0)); SB(); LDV(V1, voff, UOFF(1, 0)); SB();
    const f32x4 qa = diag_proj(W, xr, c);
#pragma unroll
    for (int i = 0; i < 4; ++i) sm.qpart[blk][c >> 3][8 * (4 * g + i) + (c & 7)] = qa[i];
  }
  auto ro = __builtin_amdgcn_make_buffer_rsrc((void*)wo, 0, HD * D * 2, 0x00020000);
  const int ovo = (h * DH + 32 * g) * (D * 2) + wave * 256 + c * 16;
#define LDWO() _Pragma("unroll") for (int i = 0; i < 32; ++i) { W[i] = __builtin_amdgcn_raw_buffer_load_b128(ro, ovo, i * (D * 2), WO_AUX); asm volatile("" ::: "memory"); }
  SB();
  BAR();
  if (wave == 0) STAMP(1);
  if (wave == 0) {
    asm volatile("s_waitcnt vmcnt(0)" ::: "memory");
    tag += 1;
    if (lane == 0) __hip_atomic_store(reinterpret_cast<u64*>(ws + WS_FLAGS) + blockIdx.x, tag, __ATOMIC_RELAXED, __HIP_MEMORY_SCOPE_AGENT);
  }
  SB();
  constexpr float C2 = 0.088388347648318440f * 1.4426950408889634f;
  u32x4 q[2];
#pragma unroll
  for (int item = 0; item < 2; ++item) {
    float qf[8];
#pragma unroll
    for (int k = 0; k < 8; ++k) qf[k] = 0.f;
#pragma unroll
    for (int s = 0; s < 8; ++s) {
      f32x4 a = *reinterpret_cast<const f32x4*>(&sm.qpart[s][item][8 * c]);
      f32x4 bb = *reinterpret_cast<const f32x4*>(&sm.qpart[s][item][8 * c + 4]);
#pragma unroll
      for (int k = 0; k < 4; ++k) { qf[k] += a[k]; qf[4 + k] += bb[k]; }
    }
#pragma unroll
    for (int k = 0; k < 4; ++k) q[item][k] = pk2(qf[2 * k], qf[2 * k + 1]);
  }
  float m[2] = {-INFINITY, -INFINITY}, l[2] = {0.f, 0.f}, acc[2][8];
#pragma unroll
  for (int k = 0; k < 8; ++k) acc[0][k] = acc[1][k] = 0.f;
#define UNIT(I, KR, VR) do { \
      const unsigned so_ = u < NU ? UOFF(I, u) : 0u; \
      float s_[4]; \
      _Pragma("unroll") for (int t = 0; t < 4; ++t) { float d_ = 0.f; \
        _Pragma("unroll") for (int k = 0; k < 4; ++k) d_ = dot2(KR[t][k], q[I][k], d_); s_[t] = d_; } \
      asm volatile("" : "+v"(s_[0]), "+v"(s_[1]), "+v"(s_[2]), "+v"(s_[3]) :: "memory"); SB(); \
      LDK(KR, vo_, so_); SB(); \
      _Pragma("unroll") for (int t = 0; t < 4; ++t) { float d_ = s_[t]; d_ += dpp<XOR1>(d_); d_ += dpp<XOR2>(d_); d_ += dpp<XOR7>(d_); d_ += dpp<XOR8>(d_); s_[t] = d_ * C2; } \
      const float mn_ = fmaxf(fmaxf(m[I], fmaxf(s_[0], s_[1])), fmaxf(s_[2], s_[3])); \
      const float al_ = __builtin_amdgcn_exp2f(m[I] - mn_); m[I] = mn_; \
      float p_[4]; \
      _Pragma("unroll") for (int t = 0; t < 4; ++t) p_[t] = __builtin_amdgcn_exp2f(s_[t] - mn_); \
      l[I] = l[I] * al_ + ((p_[0] + p_[1]) + (p_[2] + p_[3])); \
      _Pragma("unroll") for (int k = 0; k < 8; ++k) acc[I][k] *= al_; \
      _Pragma("unroll") for (int t = 0; t < 4; ++t) \
        _Pragma("unroll") for (int k = 0; k < 4; ++k) { acc[I][2 * k] = fmaf(p_[t], bflo(VR[t][k]), acc[I][2 * k]); acc[I][2 * k + 1] = fmaf(p_[t], bfhi(VR[t][k]), acc[I][2 * k + 1]); } \
      asm volatile("" : "+v"(acc[I][0]), "+v"(acc[I][1]), "+v"(acc[I][2]), "+v"(acc[I][3]), "+v"(acc[I][4]), "+v"(acc[I][5]), "+v"(acc[I][6]), "+v"(acc[I][7]) :: "memory"); SB(); \
      LDV(VR, vo_, so_); SB(); \
    } while (0)
  int u = 1;
  const int usplit = USP0 + USP1 * (j & 3);
#pragma unroll 1
  do {
    const int vo_ = voff;
    UNIT(0, K0, V0);
    UNIT(1, K1, V1);
    ++u;
  } while (u <= usplit);
  LDWO(); SB();
#pragma unroll 1
  do {
    const int vo_ = u < NU ? voff : 0x7ff00000;
    UNIT(0, K0, V0);
    UNIT(1, K1, V1);
    ++u;
  } while (u <= NU);
  {
    const float M0 = rowmax(m[0]), M1 = rowmax(m[1]);
    const float f0 = __builtin_amdgcn_exp2f(m[0] - M0), f1 = __builtin_amdgcn_exp2f(m[1] - M1);
    float s[8];
#pragma unroll
    for (int k = 0; k < 8; ++k) s[k] = fold32(acc[0][k] * f0, acc[1][k] * f1);
    f32x4 t;
#pragma unroll
    for (int k = 0; k < 4; ++k) t[k] = fold16(s[k], s[k + 4]);
    float ls = fold32(l[0] * f0, l[1] * f1);
    ls = fold16(ls, ls);
    *reinterpret_cast<f32x4*>(&sm.pacc[g >> 1][wave][8 * c + 4 * (g & 1)]) = t;
    if ((lane & 31) == 0) { sm.pm[g >> 1][wave] = g ? M1 : M0; sm.pl[g >> 1][wave] = ls; }
  }
  if (wave == 0) {
    STAMP(2);
    int spins = 0;
    while (true) {
      asm volatile("" ::: "memory");
      const u32x4 fa = __builtin_amdgcn_raw_buffer_load_b128(rs, WS_FLAGS + lane * 16, 0, SC1);
      const u32x4 fb = __builtin_amdgcn_raw_buffer_load_b128(rs, WS_FLAGS + lane * 16, 1024, SC1);
      const unsigned tl = (unsigned)tag, th = (unsigned)(tag >> 32);
      const bool ok = fa[0] == tl && fa[1] == th && fa[2] == tl && fa[3] == th && fb[0] == tl && fb[1] == th && fb[2] == tl && fb[3] == th;
      if (__builtin_amdgcn_ballot_w64(!ok) == 0 || ++spins > (1 << 20)) break;
      __builtin_amdgcn_s_sleep(8);
    }
    if (blockIdx.x == 0 && lane == 0) __hip_atomic_store(reinterpret_cast<u64*>(ws + WS_EPOCH), tag, __ATOMIC_RELAXED, __HIP_MEMORY_SCOPE_AGENT);
  }
  BAR();
  if (wave == 0) STAMP(3);
  if (tid < 256) {
    const int item = tid >> 7, d = tid & 127;
    const f32x4 ma = *reinterpret_cast<const f32x4*>(&sm.pm[item][0]), mb = *reinterpret_cast<const f32x4*>(&sm.pm[item][4]);
    const f32x4 la = *reinterpret_cast<const f32x4*>(&sm.pl[item][0]), lb = *reinterpret_cast<const f32x4*>(&sm.pl[item][4]);
    float a[8];
#pragma unroll
    for (int w = 0; w < 8; ++w) a[w] = sm.pacc[item][w][d];
    const float M = fmaxf(fmaxf(fmaxf(ma[0], ma[1]), fmaxf(ma[2], ma[3])), fmaxf(fmaxf(mb[0], mb[1]), fmaxf(mb[2], mb[3])));
    float L = 0.f, O = 0.f;
#pragma unroll
    for (int w = 0; w < 8; ++w) { const float f = __builtin_amdgcn_exp2f((w < 4 ? ma[w & 3] : mb[w & 3]) - M); L = fmaf(f, w < 4 ? la[w & 3] : lb[w & 3], L); O = fmaf(f, a[w], O); }
    sm.o[item][d] = f2bf(O / L);
  }
  BAR();
  {
    u32x4 xr[4];
#pragma unroll
    for (int i = 0; i < 4; ++i) xr[i] = *reinterpret_cast<const u32x4*>(&sm.o[c >> 3][32 * g + 8 * i]);
    const f32x4 pa = diag_proj(W, xr, c);
    u32x4 pv;
#pragma unroll
    for (int i = 0; i < 4; ++i) { const unsigned b = asu(pa[i]); pv[i] = b == SENT ? 0x7fc00000u : b; }
    __builtin_amdgcn_raw_buffer_store_b128(pv, rs, boxo, wave * 4 * 8192, SC1);
  }
  if (wave == 0) {
    STAMP(4);
    const int ib = WS_BOX + blockIdx.x * 8192;
    u32x4 v[8];
    int spins = 0;
    while (true) {
      asm volatile("" ::: "memory");
#pragma unroll
      for (int k = 0; k < 8; ++k) v[k] = __builtin_amdgcn_raw_buffer_load_b128(rs, lane * 16 + k * 1024, ib, SC1);
      bool bad = false;
#pragma unroll
      for (int k = 0; k < 8; ++k) bad |= v[k][0] == SENT || v[k][1] == SENT || v[k][2] == SENT || v[k][3] == SENT;
      if (__builtin_amdgcn_ballot_w64(bad) == 0 || ++spins > (1 << 20)) break;
      __builtin_amdgcn_s_sleep(2);
    }
    float r[4];
#pragma unroll
    for (int i = 0; i < 4; ++i) {
      float s = asf(v[0][i]);
#pragma unroll
      for (int k = 1; k < 8; ++k) s += asf(v[k][i]);
      r[i] = s;
    }
    const float y = fold16(fold32(r[0], r[2]), fold32(r[1], r[3]));
    const int obp = blockIdx.x >> 5, p = blockIdx.x & 31;
    out[(2 * obp + (c >> 3)) * D + 32 * p + 8 * g + (c & 7)] = f2bf(y);
    STAMP(5);
  }
}

extern "C" void kernel_launch(void* const* d_in, const int* in_sizes, int n_in, void* d_out, int out_size, void* d_ws, size_t ws_size, hipStream_t stream) {
  const u16* x = (const u16*)d_in[0];
  const u16* kc = (const u16*)d_in[1];
  const u16* vc = (const u16*)d_in[2];
  const u16* wq = (const u16*)d_in[3];
  const u16* wo = (const u16*)d_in[4];
  mha_fused<<<256, 512, 0, stream>>>(x, kc, vc, wq, wo, (u16*)d_out, (char*)d_ws, (u64*)((char*)d_ws + (512ull << 20)));
}
```

```cpp
#include <hip/hip_runtime.h>
#include <stdint.h>

typedef unsigned u32x4 __attribute__((ext_vector_type(4)));
typedef float f32x4 __attribute__((ext_vector_type(4)));
typedef __attribute__((ext_vector_type(2))) __bf16 bf16x2;
typedef __attribute__((ext_vector_type(8))) __bf16 bf16x8;
typedef unsigned short u16;
typedef unsigned long long u64;

constexpr int B = 16, D = 1024, H = 32, SK = 2048, DH = 128, HD = H * DH;
#define WQ_AUX 2
#define WO_AUX 2
#define WO_MODE 1
#define USP0 2
#define USP1 3
#define PT0 300
#define PDT 462
#define PMASK 0xff
#define PACE(hs) do { if (PDT && u < NU && ((PMASK >> xcd) & 1)) { const unsigned tt_ = PT0 + ((hs) * PDT + wave * (PDT / 8)) / 2; while ((unsigned)__builtin_amdgcn_s_memrealtime() - tstart < tt_) __builtin_amdgcn_s_sleep(1); } SB(); } while (0)
#define STAG 2

constexpr unsigned WS_EPOCH = 0;
constexpr unsigned WS_FLAGS = 4096;
constexpr unsigned WS_BOX = 8192;
constexpr unsigned WS_BYTES = WS_BOX + 256 * 8192;
constexpr unsigned SENT = 0xffffffffu;
constexpr int SC1 = 16;

__device__ __forceinline__ float dot2(unsigned k, unsigned q, float acc) {
  return __builtin_amdgcn_fdot2_f32_bf16(__builtin_bit_cast(bf16x2, k), __builtin_bit_cast(bf16x2, q), acc, false);
}
__device__ __forceinline__ float asf(unsigned u) { return __builtin_bit_cast(float, u); }
__device__ __forceinline__ unsigned asu(float f) { return __builtin_bit_cast(unsigned, f); }
__device__ __forceinline__ float bflo(unsigned u) { return asf(u << 16); }
__device__ __forceinline__ float bfhi(unsigned u) { return asf(u & 0xffff0000u); }
__device__ __forceinline__ u16 f2bf(float f) { __bf16 b = (__bf16)f; return __builtin_bit_cast(u16, b); }
__device__ __forceinline__ unsigned pk2(float lo, float hi) { bf16x2 v = {(__bf16)lo, (__bf16)hi}; return __builtin_bit_cast(unsigned, v); }
template <int CTRL> __device__ __forceinline__ float dpp(float x) {
  return __builtin_bit_cast(float, __builtin_amdgcn_mov_dpp(__builtin_bit_cast(int, x), CTRL, 0xf, 0xf, true));
}
__device__ __forceinline__ float fold32(float a, float b) { auto r = __builtin_amdgcn_permlane32_swap(asu(a), asu(b), false, false); return asf(r[0]) + asf(r[1]); }
__device__ __forceinline__ float fold16(float a, float b) { auto r = __builtin_amdgcn_permlane16_swap(asu(a), asu(b), false, false); return asf(r[0]) + asf(r[1]); }
__device__ __forceinline__ float rowmax(float x) {
  auto r = __builtin_amdgcn_permlane16_swap(asu(x), asu(x), false, false); x = fmaxf(asf(r[0]), asf(r[1]));
  auto s = __builtin_amdgcn_permlane32_swap(asu(x), asu(x), false, false); return fmaxf(asf(s[0]), asf(s[1]));
}
constexpr int XOR1 = 0xB1, XOR2 = 0x4E, XOR7 = 0x141, XOR8 = 0x128;
#define SB() __builtin_amdgcn_sched_barrier(0)
#define BAR() asm volatile("s_waitcnt lgkmcnt(0)\n\ts_barrier" ::: "memory")

#define STAMP(k) do {} while (0)

struct Smem {
  float qpart[8][2][128];
  float pacc[2][8][128];
  float pm[2][8], pl[2][8];
  u16 o[2][128];
};

__device__ __forceinline__ f32x4 diag_proj(const u32x4 (&W)[32], const u32x4 (&xr)[4], int c) {
  unsigned sel[2][4];
#pragma unroll
  for (int par = 0; par < 2; ++par)
#pragma unroll
    for (int r = 0; r < 4; ++r) {
      const unsigned src = par ? 0x0302u : 0x0100u;
      const unsigned v = (c & 1) ? ((src << 16) | 0x0c0cu) : (0x0c0c0000u | src);
      sel[par][r] = (r == ((c & 7) >> 1)) ? v : 0x0c0c0c0cu;
    }
  f32x4 acc0 = {0.f, 0.f, 0.f, 0.f}, acc1 = {0.f, 0.f, 0.f, 0.f};
#pragma unroll
  for (int i = 0; i < 32; ++i) {
    const unsigned xv = xr[i >> 3][(i >> 1) & 3];
    u32x4 bo;
#pragma unroll
    for (int r = 0; r < 4; ++r) bo[r] = __builtin_amdgcn_perm(xv, xv, sel[i & 1][r]);
    if (i & 1) acc1 = __builtin_amdgcn_mfma_f32_16x16x32_bf16(__builtin_bit_cast(bf16x8, W[i]), __builtin_bit_cast(bf16x8, bo), acc1, 0, 0, 0);
    else acc0 = __builtin_amdgcn_mfma_f32_16x16x32_bf16(__builtin_bit_cast(bf16x8, W[i]), __builtin_bit_cast(bf16x8, bo), acc0, 0, 0, 0);
    SB();
  }
  return acc0 + acc1;
}

__global__ void __launch_bounds__(512, 2) mha_fused(const u16* x, const u16* kc, const u16* vc, const u16* wq, const u16* wo, u16* out, char* ws, u64* stamps) {
  __shared__ __attribute__((aligned(16))) Smem sm;
  const int tid = threadIdx.x, lane = tid & 63;
  const int wave = __builtin_amdgcn_readfirstlane(tid >> 6);
  const int xcd = blockIdx.x & 7, j = blockIdx.x >> 3;
  const int h = 4 * xcd + (j & 3), bp = j >> 2, b0 = 2 * bp;
  const int g = lane >> 4, c = lane & 15;
  if (wave == 0) STAMP(0);
  const unsigned tstart = (unsigned)__builtin_amdgcn_s_memrealtime();
  auto rk = __builtin_amdgcn_make_buffer_rsrc((void*)kc, 0, B * H * SK * DH * 2, 0x00020000);
  auto rv = __builtin_amdgcn_make_buffer_rsrc((void*)vc, 0, B * H * SK * DH * 2, 0x00020000);
  auto rs = __builtin_amdgcn_make_buffer_rsrc((void*)ws, 0, WS_BYTES, 0x00020000);
  const int voff = lane * 16;
  constexpr unsigned ITEM_STRIDE = H * SK * DH * 2;
  constexpr int NU = 16;
  const unsigned hsh = (blockIdx.x * 0x9E3779B1u) >> 20;
  const int rot = STAG >= 2 ? (hsh & (NU - 1)) : 0;
  const unsigned base0 = (unsigned)(b0 * H + h) * (SK * DH * 2) + wave * 4096;
#define UOFF(it, u) (base0 + (it) * ITEM_STRIDE + (((u) + rot) & (NU - 1)) * 32768)
  u32x4 K0[4], V0[4], K1[4], V1[4];
#define LDK(R, vo, so) _Pragma("unroll") for (int t_ = 0; t_ < 4; ++t_) { R[t_] = __builtin_amdgcn_raw_buffer_load_b128(rk, (vo) + t_ * 1024, (so), 2); asm volatile("" ::: "memory"); }
#define LDV(R, vo, so) _Pragma("unroll") for (int t_ = 0; t_ < 4; ++t_) { R[t_] = __builtin_amdgcn_raw_buffer_load_b128(rv, (vo) + t_ * 1024, (so), 2); asm volatile("" ::: "memory"); }
  const int boxo = WS_BOX + (bp * 32 + g) * 8192 + h * 256 + c * 16;
  u64 tag = 0;
  if (wave == 0) {
    tag = __hip_atomic_load(reinterpret_cast<u64*>(ws + WS_EPOCH), __ATOMIC_RELAXED, __HIP_MEMORY_SCOPE_AGENT);
    const u32x4 sv = {SENT, SENT, SENT, SENT};
#pragma unroll
    for (int w = 0; w < 8; ++w) __builtin_amdgcn_raw_buffer_store_b128(sv, rs, boxo, w * 4 * 8192, SC1);
    asm volatile("" ::: "memory");
  }
  SB();
  u32x4 W[32];
  {
    const int blk = (wave + bp) & 7;
    const int krow0 = 128 * blk + 32 * g;
    auto rw = __builtin_amdgcn_make_buffer_rsrc((void*)wq, 0, D * HD * 2, 0x00020000);
    const int wvo = krow0 * (HD * 2) + h * (DH * 2) + c * 16;
    u32x4 xr[4];
    const u16* xp = x + (b0 + (c >> 3)) * D + krow0;
#pragma unroll
    for (int i = 0; i < 4; ++i) xr[i] = *reinterpret_cast<const u32x4*>(xp + 8 * i);
    SB();
#pragma unroll
    for (int i = 0; i < 32; ++i) { W[i] = __builtin_amdgcn_raw_buffer_load_b128(rw, wvo, i * (HD * 2), WQ_AUX); asm volatile("" ::: "memory"); }
    SB();
    LDK(K0, voff, UOFF(0, 0)); SB(); LDV(V0, voff, UOFF(0, 0)); SB();
    LDK(K1, voff, UOFF(1, 0)); SB(); LDV(V1, voff, UOFF(1, 0)); SB();
    const f32x4 qa = diag_proj(W, xr, c);
#pragma unroll
    for (int i = 0; i < 4; ++i) sm.qpart[blk][c >> 3][8 * (4 * g + i) + (c & 7)] = qa[i];
  }
  auto ro = __builtin_amdgcn_make_buffer_rsrc((void*)wo, 0, HD * D * 2, 0x00020000);
  const int ovo = (h * DH + 32 * g) * (D * 2) + wave * 256 + c * 16;
#define LDWO() _Pragma("unroll") for (int i = 0; i < 32; ++i) { W[i] = __builtin_amdgcn_raw_buffer_load_b128(ro, ovo, i * (D * 2), WO_AUX); asm volatile("" ::: "memory"); }
  SB();
  BAR();
  if (wave == 0) STAMP(1);
  if (wave == 0) {
    asm volatile("s_waitcnt vmcnt(0)" ::: "memory");
    tag += 1;
    if (lane == 0) __hip_atomic_store(reinterpret_cast<u64*>(ws + WS_FLAGS) + blockIdx.x, tag, __ATOMIC_RELAXED, __HIP_MEMORY_SCOPE_AGENT);
  }
  SB();
  constexpr float C2 = 0.088388347648318440f * 1.4426950408889634f;
  u32x4 q[2];
#pragma unroll
  for (int item = 0; item < 2; ++item) {
    float qf[8];
#pragma unroll
    for (int k = 0; k < 8; ++k) qf[k] = 0.f;
#pragma unroll
    for (int s = 0; s < 8; ++s) {
      f32x4 a = *reinterpret_cast<const f32x4*>(&sm.qpart[s][item][8 * c]);
      f32x4 bb = *reinterpret_cast<const f32x4*>(&sm.qpart[s][item][8 * c + 4]);
#pragma unroll
      for (int k = 0; k < 4; ++k) { qf[k] += a[k]; qf[4 + k] += bb[k]; }
    }
#pragma unroll
    for (int k = 0; k < 4; ++k) q[item][k] = pk2(qf[2 * k], qf[2 * k + 1]);
  }
  float m[2] = {-INFINITY, -INFINITY}, l[2] = {0.f, 0.f}, acc[2][8];
#pragma unroll
  for (int k = 0; k < 8; ++k) acc[0][k] = acc[1][k] = 0.f;
#define UNIT(I, KR, VR) do { \
      const unsigned so_ = u < NU ? UOFF(I, u) : 0u; \
      float s_[4]; \
      _Pragma("unroll") for (int t = 0; t < 4; ++t) { float d_ = 0.f; \
        _Pragma("unroll") for (int k = 0; k < 4; ++k) d_ = dot2(KR[t][k], q[I][k], d_); s_[t] = d_; } \
      asm volatile("" : "+v"(s_[0]), "+v"(s_[1]), "+v"(s_[2]), "+v"(s_[3]) :: "memory"); SB(); \
      LDK(KR, vo_, so_); SB(); \
      _Pragma("unroll") for (int t = 0; t < 4; ++t) { float d_ = s_[t]; d_ += dpp<XOR1>(d_); d_ += dpp<XOR2>(d_); d_ += dpp<XOR7>(d_); d_ += dpp<XOR8>(d_); s_[t] = d_ * C2; } \
      const float mn_ = fmaxf(fmaxf(m[I], fmaxf(s_[0], s_[1])), fmaxf(s_[2], s_[3])); \
      const float al_ = __builtin_amdgcn_exp2f(m[I] - mn_); m[I] = mn_; \
      float p_[4]; \
      _Pragma("unroll") for (int t = 0; t < 4; ++t) p_[t] = __builtin_amdgcn_exp2f(s_[t] - mn_); \
      l[I] = l[I] * al_ + ((p_[0] + p_[1]) + (p_[2] + p_[3])); \
      _Pragma("unroll") for (int k = 0; k < 8; ++k) acc[I][k] *= al_; \
      _Pragma("unroll") for (int t = 0; t < 4; ++t) \
        _Pragma("unroll") for (int k = 0; k < 4; ++k) { acc[I][2 * k] = fmaf(p_[t], bflo(VR[t][k]), acc[I][2 * k]); acc[I][2 * k + 1] = fmaf(p_[t], bfhi(VR[t][k]), acc[I][2 * k + 1]); } \
      asm volatile("" : "+v"(acc[I][0]), "+v"(acc[I][1]), "+v"(acc[I][2]), "+v"(acc[I][3]), "+v"(acc[I][4]), "+v"(acc[I][5]), "+v"(acc[I][6]), "+v"(acc[I][7]) :: "memory"); SB(); \
      LDV(VR, vo_, so_); SB(); \
    } while (0)
  int u = 1;
  const int usplit = USP0 + USP1 * (j & 3);
#pragma unroll 1
  do {
    const int vo_ = voff;
    PACE(2 * u); UNIT(0, K0, V0);
    PACE(2 * u + 1); UNIT(1, K1, V1);
    ++u;
  } while (u <= usplit);
  LDWO(); SB();
#pragma unroll 1
  do {
    const int vo_ = u < NU ? voff : 0x7ff00000;
    PACE(2 * u); UNIT(0, K0, V0);
    PACE(2 * u + 1); UNIT(1, K1, V1);
    ++u;
  } while (u <= NU);
  {
    const float M0 = rowmax(m[0]), M1 = rowmax(m[1]);
    const float f0 = __builtin_amdgcn_exp2f(m[0] - M0), f1 = __builtin_amdgcn_exp2f(m[1] - M1);
    float s[8];
#pragma unroll
    for (int k = 0; k < 8; ++k) s[k] = fold32(acc[0][k] * f0, acc[1][k] * f1);
    f32x4 t;
#pragma unroll
    for (int k = 0; k < 4; ++k) t[k] = fold16(s[k], s[k + 4]);
    float ls = fold32(l[0] * f0, l[1] * f1);
    ls = fold16(ls, ls);
    *reinterpret_cast<f32x4*>(&sm.pacc[g >> 1][wave][8 * c + 4 * (g & 1)]) = t;
    if ((lane & 31) == 0) { sm.pm[g >> 1][wave] = g ? M1 : M0; sm.pl[g >> 1][wave] = ls; }
  }
  if (wave == 0) {
    STAMP(2);
    int spins = 0;
    while (true) {
      asm volatile("" ::: "memory");
      const u32x4 fa = __builtin_amdgcn_raw_buffer_load_b128(rs, WS_FLAGS + lane * 16, 0, SC1);
      const u32x4 fb = __builtin_amdgcn_raw_buffer_load_b128(rs, WS_FLAGS + lane * 16, 1024, SC1);
      const unsigned tl = (unsigned)tag, th = (unsigned)(tag >> 32);
      const bool ok = fa[0] == tl && fa[1] == th && fa[2] == tl && fa[3] == th && fb[0] == tl && fb[1] == th && fb[2] == tl && fb[3] == th;
      if (__builtin_amdgcn_ballot_w64(!ok) == 0 || ++spins > (1 << 20)) break;
      __builtin_amdgcn_s_sleep(8);
    }
    if (blockIdx.x == 0 && lane == 0) __hip_atomic_store(reinterpret_cast<u64*>(ws + WS_EPOCH), tag, __ATOMIC_RELAXED, __HIP_MEMORY_SCOPE_AGENT);
  }
  BAR();
  if (wave == 0) STAMP(3);
  if (tid < 256) {
    const int item = tid >> 7, d = tid & 127;
    const f32x4 ma = *reinterpret_cast<const f32x4*>(&sm.pm[item][0]), mb = *reinterpret_cast<const f32x4*>(&sm.pm[item][4]);
    const f32x4 la = *reinterpret_cast<const f32x4*>(&sm.pl[item][0]), lb = *reinterpret_cast<const f32x4*>(&sm.pl[item][4]);
    float a[8];
#pragma unroll
    for (int w = 0; w < 8; ++w) a[w] = sm.pacc[item][w][d];
    const float M = fmaxf(fmaxf(fmaxf(ma[0], ma[1]), fmaxf(ma[2], ma[3])), fmaxf(fmaxf(mb[0], mb[1]), fmaxf(mb[2], mb[3])));
    float L = 0.f, O = 0.f;
#pragma unroll
    for (int w = 0; w < 8; ++w) { const float f = __builtin_amdgcn_exp2f((w < 4 ? ma[w & 3] : mb[w & 3]) - M); L = fmaf(f, w < 4 ? la[w & 3] : lb[w & 3], L); O = fmaf(f, a[w], O); }
    sm.o[item][d] = f2bf(O / L);
  }
  BAR();
  {
    u32x4 xr[4];
#pragma unroll
    for (int i = 0; i < 4; ++i) xr[i] = *reinterpret_cast<const u32x4*>(&sm.o[c >> 3][32 * g + 8 * i]);
    const f32x4 pa = diag_proj(W, xr, c);
    u32x4 pv;
#pragma unroll
    for (int i = 0; i < 4; ++i) { const unsigned b = asu(pa[i]); pv[i] = b == SENT ? 0x7fc00000u : b; }
    __builtin_amdgcn_raw_buffer_store_b128(pv, rs, boxo, wave * 4 * 8192, SC1);
  }
  if (wave == 0) {
    STAMP(4);
    const int ib = WS_BOX + blockIdx.x * 8192;
    u32x4 v[8];
    int spins = 0;
    while (true) {
      asm volatile("" ::: "memory");
#pragma unroll
      for (int k = 0; k < 8; ++k) v[k] = __builtin_amdgcn_raw_buffer_load_b128(rs, lane * 16 + k * 1024, ib, SC1);
      bool bad = false;
#pragma unroll
      for (int k = 0; k < 8; ++k) bad |= v[k][0] == SENT || v[k][1] == SENT || v[k][2] == SENT || v[k][3] == SENT;
      if (__builtin_amdgcn_ballot_w64(bad) == 0 || ++spins > (1 << 20)) break;
      __builtin_amdgcn_s_sleep(2);
    }
    float r[4];
#pragma unroll
    for (int i = 0; i < 4; ++i) {
      float s = asf(v[0][i]);
#pragma unroll
      for (int k = 1; k < 8; ++k) s += asf(v[k][i]);
      r[i] = s;
    }
    const float y = fold16(fold32(r[0], r[2]), fold32(r[1], r[3]));
    const int obp = blockIdx.x >> 5, p = blockIdx.x & 31;
    out[(2 * obp + (c >> 3)) * D + 32 * p + 8 * g + (c & 7)] = f2bf(y);
    STAMP(5);
  }
}

extern "C" void kernel_launch(void* const* d_in, const int* in_sizes, int n_in, void* d_out, int out_size, void* d_ws, size_t ws_size, hipStream_t stream) {
  const u16* x = (const u16*)d_in[0];
  const u16* kc = (const u16*)d_in[1];
  const u16* vc = (const u16*)d_in[2];
  const u16* wq = (const u16*)d_in[3];
  const u16* wo = (const u16*)d_in[4];
  mha_fused<<<256, 512, 0, stream>>>(x, kc, vc, wq, wo, (u16*)d_out, (char*)d_ws, (u64*)((char*)d_ws + (512ull << 20)));
}
```

```cpp
#include <hip/hip_runtime.h>
#include <stdint.h>

typedef unsigned u32x4 __attribute__((ext_vector_type(4)));
typedef unsigned u32x2 __attribute__((ext_vector_type(2)));
typedef float f32x4 __attribute__((ext_vector_type(4)));
typedef float f32x2 __attribute__((ext_vector_type(2)));
typedef __attribute__((ext_vector_type(2))) __bf16 bf16x2;
typedef unsigned short u16;
typedef unsigned long long u64;

constexpr int B = 16, D = 1024, H = 32, SK = 2048, DH = 128, HD = H * DH;
constexpr int PT0 = 390, PDT = 441, PAB = 200, PDLY = 75;
constexpr int TH0 = 70, TH1 = 100, TH2 = 20, DL1 = 40, DL2 = 90;
#define PATT 0x1,0xa,0x28, 0x2d,0x8,0xc8, 0x41,0x50,0x100, 0x100,0,0x48,0x80,0x100,0x4,0x9,0x110, 0x20, 3,5,9,7,0xb,0xa,0xd,6

constexpr unsigned WS_EPOCH = 0;
constexpr unsigned WS_SLOW = 2048;
constexpr unsigned WS_BOX = 8192;
constexpr unsigned WS_BYTES = WS_BOX + 256 * 8192;
constexpr int SC1 = 16, NT = 2;
constexpr int OOB = 0x7ff00000;

__device__ __forceinline__ float dot2(unsigned k, unsigned q, float acc) {
  return __builtin_amdgcn_fdot2_f32_bf16(__builtin_bit_cast(bf16x2, k), __builtin_bit_cast(bf16x2, q), acc, false);
}
__device__ __forceinline__ float asf(unsigned u) { return __builtin_bit_cast(float, u); }
__device__ __forceinline__ unsigned asu(float f) { return __builtin_bit_cast(unsigned, f); }
__device__ __forceinline__ float bflo(unsigned u) { return asf(u << 16); }
__device__ __forceinline__ float bfhi(unsigned u) { return asf(u & 0xffff0000u); }
__device__ __forceinline__ u16 f2bf(float f) { __bf16 b = (__bf16)f; return __builtin_bit_cast(u16, b); }
__device__ __forceinline__ unsigned pk2(float lo, float hi) { bf16x2 v = {(__bf16)lo, (__bf16)hi}; return __builtin_bit_cast(unsigned, v); }
template <int CTRL> __device__ __forceinline__ float dpp(float x) {
  return __builtin_bit_cast(float, __builtin_amdgcn_mov_dpp(__builtin_bit_cast(int, x), CTRL, 0xf, 0xf, true));
}
__device__ __forceinline__ float fold32(float a, float b) { auto r = __builtin_amdgcn_permlane32_swap(asu(a), asu(b), false, false); return asf(r[0]) + asf(r[1]); }
__device__ __forceinline__ float fold16(float a, float b) { auto r = __builtin_amdgcn_permlane16_swap(asu(a), asu(b), false, false); return asf(r[0]) + asf(r[1]); }
__device__ __forceinline__ float rowmax(float x) {
  auto r = __builtin_amdgcn_permlane16_swap(asu(x), asu(x), false, false); x = fmaxf(asf(r[0]), asf(r[1]));
  auto s = __builtin_amdgcn_permlane32_swap(asu(x), asu(x), false, false); return fmaxf(asf(s[0]), asf(s[1]));
}
constexpr int XOR1 = 0xB1, XOR2 = 0x4E, XOR7 = 0x141, XOR8 = 0x128, SHL1 = 0x101, BCAST0 = 0x150;
#define SB() __builtin_amdgcn_sched_barrier(0)
#define MEMPIN() asm volatile("" ::: "memory")
#define BAR() asm volatile("s_waitcnt lgkmcnt(0)\n\ts_barrier" ::: "memory")
#define NOW() ((unsigned)__builtin_amdgcn_s_memrealtime())

struct Smem {
  float qpart[8][2][128];
  float pacc[2][8][128];
  float pm[2][8], pl[2][8];
  u16 o[2][128];
  unsigned ep[2];
  int slip;
};

struct Sel { unsigned s[2][2]; int idx; };
__device__ __forceinline__ Sel make_sel(int c) {
  Sel r;
#pragma unroll
  for (int par = 0; par < 2; ++par)
#pragma unroll
    for (int q = 0; q < 2; ++q) r.s[par][q] = (q == ((c >> 2) & 1)) ? (0x0c0c0000u | (par ? 0x0302u : 0x0100u)) : 0x0c0c0c0cu;
  r.idx = (c & 3) * 0x5555;
  return r;
}
template <int PEND, int I> __device__ __forceinline__ void proj_step(f32x4& acc, const u32x4 (&xr)[4], const Sel& sl) {
  const unsigned xv = xr[I >> 3][(I >> 1) & 3], yv = xr[2 + (I >> 3)][(I >> 1) & 3];
  const u32x4 a = {__builtin_amdgcn_perm(xv, xv, sl.s[I & 1][0]), __builtin_amdgcn_perm(xv, xv, sl.s[I & 1][1]),
                   __builtin_amdgcn_perm(yv, yv, sl.s[I & 1][0]), __builtin_amdgcn_perm(yv, yv, sl.s[I & 1][1])};
  if (PEND >= 0)
    asm volatile("s_waitcnt vmcnt(%5)\n\ts_nop 0\n\tv_smfmac_f32_16x16x64_bf16 %0, %1, a[%3:%4], %2"
                 : "+v"(acc) : "v"(a), "v"(sl.idx), "n"(8 * I), "n"(8 * I + 7), "n"(PEND >= 0 ? PEND + 30 - 2 * I : 0));
  else
    asm volatile("s_nop 1\n\tv_smfmac_f32_16x16x64_bf16 %0, %1, a[%3:%4], %2" : "+v"(acc) : "v"(a), "v"(sl.idx), "n"(8 * I), "n"(8 * I + 7));
}
template <int PEND> __device__ __forceinline__ f32x4 diag_proj(const u32x4 (&xr)[4], const Sel& sl) {
  f32x4 acc0 = {0.f, 0.f, 0.f, 0.f}, acc1 = {0.f, 0.f, 0.f, 0.f};
#define PS(i) proj_step<PEND, 2 * i>(acc0, xr, sl); proj_step<PEND, 2 * i + 1>(acc1, xr, sl);
  PS(0) PS(1) PS(2) PS(3) PS(4) PS(5) PS(6) PS(7)
  asm volatile("s_nop 7\n\ts_nop 1" : "+v"(acc0), "+v"(acc1));
  return acc0 + acc1;
}
template <int I> __device__ __forceinline__ void ldw(const u32x4& rsrc, int vo, int so) {
  asm volatile("buffer_load_dwordx4 a[%3:%4], %0, %1, %2 offen nt" :: "v"(vo), "s"(rsrc), "s"(so), "n"(4 * I), "n"(4 * I + 3) : "memory");
}
#define WE(a0, a1) "buffer_load_dwordx4 a[" #a0 ":" #a1 "], %0, %1, %2 offen nt\n\ts_branch .Lwend%=\n\t"
__device__ __forceinline__ void ldw_dyn(const u32x4& rsrc, int vo, int so, int i) {
  asm volatile("s_getpc_b64 vcc\n\ts_add_u32 vcc_lo, vcc_lo, %3\n\ts_addc_u32 vcc_hi, vcc_hi, 0\n\ts_setpc_b64 vcc\n\t"
               WE(0, 3) WE(4, 7) WE(8, 11) WE(12, 15) WE(16, 19) WE(20, 23) WE(24, 27) WE(28, 31)
               WE(32, 35) WE(36, 39) WE(40, 43) WE(44, 47) WE(48, 51) WE(52, 55) WE(56, 59) WE(60, 63)
               WE(64, 67) WE(68, 71) WE(72, 75) WE(76, 79) WE(80, 83) WE(84, 87) WE(88, 91) WE(92, 95)
               WE(96, 99) WE(100, 103) WE(104, 107) WE(108, 111) WE(112, 115) WE(116, 119) WE(120, 123) WE(124, 127)
               ".Lwend%=:"
               :: "v"(vo), "s"(rsrc), "s"(so), "s"(12 * i + 12) : "vcc", "scc", "memory");
}
__device__ __forceinline__ u32x4 make_rsrc(const void* p, unsigned bytes) {
  const u64 a = (u64)p;
  return u32x4{(unsigned)a, (unsigned)(a >> 32) & 0xffffu, bytes, 0x00020000u};
}
__device__ constexpr int wrow(int i) { return 16 * (i & 1) + (i >> 1); }
constexpr unsigned PV[26] = {PATT};
template <int O, int N> __device__ __forceinline__ constexpr unsigned mix(unsigned v) {
  unsigned r = 0;
#pragma unroll
  for (int i = 0; i < N; ++i) r ^= ((v >> i) & 1) ? PV[O + i] : 0u;
  return r;
}

__global__ void __launch_bounds__(512, 2) mha_fused(const u16* x, const u16* kc, const u16* vc, const u16* wq, const u16* wo, u16* out, char* ws) {
  __shared__ __attribute__((aligned(16))) Smem sm;
  const int tid = threadIdx.x, lane = tid & 63;
  const int wave = __builtin_amdgcn_readfirstlane(tid >> 6);
  const int xcd = blockIdx.x & 7, j = blockIdx.x >> 3;
  const int h = 4 * xcd + (j & 3), bp = j >> 2, b0 = 2 * bp;
  const int g = lane >> 4, c = lane & 15;
  const bool grpA = j & 1;
  const unsigned tstart = NOW();
  auto rk = __builtin_amdgcn_make_buffer_rsrc((void*)kc, 0, B * H * SK * DH * 2, 0x00020000);
  auto rv = __builtin_amdgcn_make_buffer_rsrc((void*)vc, 0, B * H * SK * DH * 2, 0x00020000);
  auto rs = __builtin_amdgcn_make_buffer_rsrc((void*)ws, 0, WS_BYTES, 0x00020000);
  const int voff = lane * 16;
  constexpr unsigned ITEM_STRIDE = H * SK * DH * 2;
  const unsigned id = h | (bp << 5);
  const unsigned base0 = ((unsigned)(b0 * H + h) * (SK * DH * 2)) ^ ((mix<9, 8>(id) ^ mix<3, 3>(wave)) << 10);
#define SOFF(n) (base0 ^ (((n) & 8) ? ITEM_STRIDE ^ (PV[17] << 10) : 0u) ^ (mix<6, 3>(n) << 10))
  u32x4 RA[8], RB[8];
#define LD(R, n) do { const unsigned so_ = SOFF(n); _Pragma("unroll") for (int t_ = 0; t_ < 8; ++t_) { \
      R[t_] = __builtin_amdgcn_raw_buffer_load_b128(rk, voff, so_ ^ (mix<0, 3>(t_) << 10), NT); MEMPIN(); } SB(); } while (0)
  const int xk = 64 * (g & 1) + 16 * (g >> 1);
#define A4(n) "a" #n "0", "a" #n "1", "a" #n "2", "a" #n "3", "a" #n "4", "a" #n "5", "a" #n "6", "a" #n "7", "a" #n "8", "a" #n "9"
  asm volatile("" ::: "a0", "a1", "a2", "a3", "a4", "a5", "a6", "a7", "a8", "a9", A4(1), A4(2), A4(3), A4(4), A4(5), A4(6), A4(7), A4(8), A4(9), A4(10), A4(11),
               "a120", "a121", "a122", "a123", "a124", "a125", "a126", "a127");
  u32x4 q[2];
  {
    const int blk = (wave + bp) & 7;
    const u32x4 rw = make_rsrc(wq, D * HD * 2);
    const int wvo = (128 * blk + 32 * g) * (HD * 2) + h * (DH * 2) + c * 16;
    u32x4 xr[4];
    const u16* xp = x + (b0 + (c >> 3)) * D + 128 * blk + xk;
    unsigned ep;
#define LW(i) ldw<i>(rw, wvo, wrow(i) * (HD * 2));
#define LW4(i) LW(4 * i) LW(4 * i + 1) LW(4 * i + 2) LW(4 * i + 3)
#define LDWQ() do { ep = __builtin_amdgcn_raw_buffer_load_b32(rs, tid == 0 ? bp * 256 : tid == 1 ? 0 : OOB, WS_EPOCH, SC1); MEMPIN(); SB(); \
      asm volatile("global_load_dwordx4 %0, %4, off\n\tglobal_load_dwordx4 %1, %4, off offset:16\n\tglobal_load_dwordx4 %2, %4, off offset:64\n\tglobal_load_dwordx4 %3, %4, off offset:80" \
                   : "=&v"(xr[0]), "=&v"(xr[1]), "=&v"(xr[2]), "=&v"(xr[3]) : "v"(xp) : "memory"); \
      LW4(0) LW4(1) LW4(2) LW4(3) LW4(4) LW4(5) LW4(6) LW4(7) SB(); } while (0)
#define QPROJ(PEND) do { \
      asm volatile("s_waitcnt vmcnt(%4)" : "+v"(xr[0]), "+v"(xr[1]), "+v"(xr[2]), "+v"(xr[3]) : "n"(PEND + 32)); \
      const f32x4 qa = diag_proj<PEND>(xr, make_sel(c)); \
      *reinterpret_cast<f32x4*>(&sm.qpart[blk][g >> 1][8 * c + 4 * (g & 1)]) = qa; \
      asm volatile("" : "+v"(ep)); \
      if (tid < 2) sm.ep[tid] = ep + 1; \
      if (tid == 2) sm.slip = 0; \
      if (tid < 16) (&sm.pl[0][0])[tid] = 0.f; \
      SB(); BAR(); \
      f32x4 qs = *reinterpret_cast<const f32x4*>(&sm.qpart[0][g >> 1][8 * c + 4 * (g & 1)]); \
      _Pragma("unroll") for (int s = 1; s < 8; ++s) qs += *reinterpret_cast<const f32x4*>(&sm.qpart[s][g >> 1][8 * c + 4 * (g & 1)]); \
      _Pragma("unroll") for (int k = 0; k < 2; ++k) { \
        const unsigned w_ = pk2(qs[2 * k], qs[2 * k + 1]); \
        const auto r_ = __builtin_amdgcn_permlane16_swap(w_, w_, false, false);        \
        const auto e_ = __builtin_amdgcn_permlane32_swap(r_[0], r_[0], false, false); \
        const auto o_ = __builtin_amdgcn_permlane32_swap(r_[1], r_[1], false, false); \
        q[0][k] = e_[0]; q[1][k] = e_[1]; q[0][2 + k] = o_[0]; q[1][2 + k] = o_[1]; \
      } \
      asm volatile("" : "+v"(q[0]), "+v"(q[1]) :: "memory"); SB(); } while (0)
    if (grpA) {
      LD(RA, 0); LD(RB, 1);
      asm volatile("s_barrier" ::: "memory"); SB();
      LDWQ();
      QPROJ(0);
      asm volatile("; cache first" ::: "memory");
    } else {
      asm volatile("; weights first" ::: "memory");
      LDWQ();
      asm volatile("s_barrier" ::: "memory"); SB();
      LD(RA, 0);
      QPROJ(8);
      LD(RB, 1);
      asm volatile("; weights first done" ::: "memory");
    }
  }
  const unsigned epoch = __builtin_amdgcn_readfirstlane(sm.ep[0]), gtag = __builtin_amdgcn_readfirstlane(sm.ep[1]), tag = epoch & 0xffu;
  SB();
  constexpr float C2 = 0.088388347648318440f * 1.4426950408889634f;
#define PACE(odd) do { \
    const int hs_ = 2 * u + (odd), tn_ = NOW() - tstart; \
    const int x_ = hs_ - 1, e_ = min(x_ + min(x_, 2), 32); \
    _Pragma("unroll 1") for (; wn < e_; ++wn) ldw_dyn(ro, ovo, wrow(wn) * (D * 2), wn); \
    const int tt_ = pt0 + ((hs_ * pdt) >> 5); \
    if (wave != 0) { \
      int sv_; asm volatile("ds_read_b32 %0, %1\n\ts_waitcnt lgkmcnt(0)" : "=v"(sv_) : "v"(slo) : "memory"); \
      slip = __builtin_amdgcn_readfirstlane(sv_); \
    } else if (u >= 3 && u <= 12) { \
      const int go_ = WS_SLOW + ((u - 1) >> 1) * 256; \
      if (odd) { \
        if (!(u & 1)) { \
          unsigned g0_, g1_; asm volatile("v_readfirstlane_b32 %0, v124\n\tv_readfirstlane_b32 %1, v125" : "=s"(g0_), "=s"(g1_) :: "memory"); \
          slip += g1_ == gtag ? DL2 : g0_ == gtag ? DL1 : 0; \
          asm volatile("ds_write_b32 %0, %1" :: "v"(slo), "v"(slip) : "memory"); \
        } \
      } else if (u & 1) { \
        const int m_ = tt_ + slip - tn_; \
        if (det && m_ < (u == 3 ? TH0 : TH1)) asm volatile("buffer_store_dword %0, %1, %2, %3 offen sc1" :: "v"(gtag), "v"(gvo), "s"(rg), "s"(m_ < TH2 ? go_ + 4 : go_) : "memory"); \
      } else asm volatile("buffer_load_dwordx2 v[124:125], %0, %1, %2 offen sc1" :: "v"(gvo), "s"(rg), "s"(go_) : "memory", "v124", "v125"); \
    } \
    while ((int)(NOW() - tstart) < tt_ + slip) __builtin_amdgcn_s_sleep(1); \
    SB(); } while (0)
  float S[8];
#pragma unroll
  for (int t = 0; t < 8; ++t) S[t] = 0.f;
#define PIN8(a) asm volatile("" : "+v"(a[0]), "+v"(a[1]), "+v"(a[2]), "+v"(a[3]), "+v"(a[4]), "+v"(a[5]), "+v"(a[6]), "+v"(a[7]) :: "memory"); SB()
#define PROCK(R, n) do { \
      const bool mine_ = c == (n); \
      _Pragma("unroll") for (int t = 0; t < 8; ++t) { float d_ = 0.f; \
        _Pragma("unroll") for (int k = 0; k < 4; ++k) d_ = dot2(R[t][k], qc[k], d_); \
        d_ += dpp<XOR1>(d_); d_ += dpp<XOR2>(d_); d_ += dpp<XOR7>(d_); d_ += dpp<XOR8>(d_); \
        S[t] = mine_ ? d_ : S[t]; } \
      PIN8(S); } while (0)
#define PROCV(R) do { \
      _Pragma("unroll") for (int t = 0; t < 8; ++t) { const float p_ = dpp<BCAST0>(S[t]); S[t] = dpp<SHL1>(S[t]); \
        _Pragma("unroll") for (int k = 0; k < 4; ++k) { acc[2 * k] = fmaf(p_, bflo(R[t][k]), acc[2 * k]); acc[2 * k + 1] = fmaf(p_, bfhi(R[t][k]), acc[2 * k + 1]); } } \
      PIN8(acc); } while (0)
#define FOLD(item, f) do { \
      float s_[4]; \
      _Pragma("unroll") for (int k = 0; k < 4; ++k) s_[k] = fold32(acc[k], acc[k + 4]); \
      const f32x2 t_ = {fold16(s_[0], s_[2]) * (f), fold16(s_[1], s_[3]) * (f)}; \
      *reinterpret_cast<f32x2*>(&sm.pacc[item][wave][8 * c + 4 * (g >> 1) + 2 * (g & 1)]) = t_; } while (0)
  int u = 1;
  const int pg0 = grpA ? PT0 + PAB : PT0 - PAB;
  const int pdt = 16 * PDT + PT0 - pg0;
  const int pt0 = pg0 + (int)((wave * 16 + mix<18, 8>(id)) * PDT) / 256;
  int wn = 0, slip = 0;
  const u32x4 rg = make_rsrc(ws, WS_BYTES);
  const int gvo = lane == 0 ? 0 : OOB, slo = (int)(size_t)(__attribute__((address_space(3))) int*)&sm.slip;
  const bool det = bp == (h & 7);
  const u32x4 ro = make_rsrc(wo, HD * D * 2);
  const int ovo = (h * DH + 32 * g) * (D * 2) + wave * 256 + c * 16;
  {
    u32x4 qc = q[0];
#pragma unroll 1
    do {
      if (u == 8) rk = rv;
      PROCK(RA, 2 * u - 2);
      PACE(0); LD(RA, 2 * u);
      PROCK(RB, 2 * u - 1);
      PACE(1); LD(RB, 2 * u + 1);
      if (u == 4) qc = q[1];
      ++u;
    } while (u <= 8);
  }
  {
    float mx = fmaxf(fmaxf(fmaxf(S[0], S[1]), fmaxf(S[2], S[3])), fmaxf(fmaxf(S[4], S[5]), fmaxf(S[6], S[7])));
    mx = fmaxf(mx, dpp<XOR1>(mx)); mx = fmaxf(mx, dpp<XOR2>(mx)); mx = fmaxf(mx, dpp<XOR7>(mx));
    mx = rowmax(mx);
    float ls = 0.f;
#pragma unroll
    for (int t = 0; t < 8; ++t) { S[t] = __builtin_amdgcn_exp2f((S[t] - mx) * C2); ls += S[t]; }
    ls += dpp<XOR1>(ls); ls += dpp<XOR2>(ls); ls += dpp<XOR7>(ls);
    ls = fold32(ls, ls); ls = fold16(ls, ls);
    if ((lane & 0x37) == 0) {
      sm.pm[lane >> 3][wave] = mx;
      MEMPIN();
      sm.pl[lane >> 3][wave] = ls;
    }
    SB();
  }
  float acc[8];
#pragma unroll
  for (int k = 0; k < 8; ++k) acc[k] = 0.f;
#pragma unroll 1
  do {
    if (u == 13) {
      float lw;
      do { MEMPIN(); lw = sm.pl[c >> 3][c & 7]; } while (__builtin_amdgcn_ballot_w64(lw == 0.f) != 0);
      MEMPIN();
      const float mw = sm.pm[c >> 3][c & 7];
      float M = fmaxf(mw, dpp<XOR1>(mw)); M = fmaxf(M, dpp<XOR2>(M)); M = fmaxf(M, dpp<XOR7>(M));
      const float e = __builtin_amdgcn_exp2f((mw - M) * C2);
      float L = e * lw;
      L += dpp<XOR1>(L); L += dpp<XOR2>(L); L += dpp<XOR7>(L);
      const unsigned f = asu(e / L);
      const float f0 = asf(__builtin_amdgcn_readlane(f, wave)), f1 = asf(__builtin_amdgcn_readlane(f, wave + 8));
      FOLD(0, f0);
#pragma unroll
      for (int k = 0; k < 8; ++k) { acc[k] = 0.f; S[k] *= f1; }
      SB();
    }
    PROCV(RA);
    PACE(0); LD(RA, 2 * u);
    PROCV(RB);
    PACE(1); LD(RB, 2 * u + 1);
    ++u;
  } while (u < 16);
  PROCV(RA);
  PROCV(RB);
  FOLD(1, 1.f);
  const int p = blockIdx.x & 31;
  const int ib = WS_BOX + blockIdx.x * 8192;
  auto rout = __builtin_amdgcn_make_buffer_rsrc((void*)out, 0, B * D * 2, 0x00020000);
  const int oo = ((b0 + ((p >> 1) & 1)) * D + 128 * (p >> 2) + 4 * (p & 1)) * 2;
  const unsigned tch0 = __builtin_amdgcn_raw_buffer_load_b32(rs, tid == 0 ? 0 : OOB, ib, SC1);
  const unsigned tch1 = __builtin_amdgcn_raw_buffer_load_b32(rout, tid == 0 ? 0 : OOB, oo, SC1);
  Sel sl = make_sel(c);
  int boxo = WS_BOX + (bp * 32 + g) * 8192 + h * 256 + c * 16;
  asm volatile("" : "+v"(sl.s[0][0]), "+v"(sl.s[0][1]), "+v"(sl.s[1][0]), "+v"(sl.s[1][1]), "+v"(sl.idx), "+v"(boxo) :: "memory");
  BAR();
  if (tid < 256) {
    const int item = tid >> 7, d = tid & 127;
    float a[8];
#pragma unroll
    for (int w = 0; w < 8; ++w) a[w] = sm.pacc[item][w][d];
    sm.o[item][d] = f2bf(((a[0] + a[1]) + (a[2] + a[3])) + ((a[4] + a[5]) + (a[6] + a[7])));
  }
  BAR();
  {
    u32x4 xr[4];
#pragma unroll
    for (int i = 0; i < 4; ++i) xr[i] = *reinterpret_cast<const u32x4*>(&sm.o[c >> 3][xk + 8 * (i & 1) + 32 * (i >> 1)]);
    const f32x4 pa = diag_proj<-1>(xr, sl);
    u32x4 pv;
#pragma unroll
    for (int i = 0; i < 4; ++i) pv[i] = ((asu(pa[i]) + 0x80u) & 0xffffff00u) | tag;
    __builtin_amdgcn_raw_buffer_store_b128(pv, rs, boxo, wave * 4 * 8192, SC1);
  }
  if (wave == 0) {
    asm volatile("" :: "v"(tch0), "v"(tch1));
    { const unsigned t4 = NOW(); while (NOW() - t4 < PDLY) __builtin_amdgcn_s_sleep(1); }
    u32x4 v[8];
    int spins = 0;
    while (true) {
      MEMPIN();
#pragma unroll
      for (int k = 0; k < 8; ++k) v[k] = __builtin_amdgcn_raw_buffer_load_b128(rs, lane * 16 + k * 1024, ib, SC1);
      unsigned bad = 0;
#pragma unroll
      for (int k = 0; k < 8; ++k) bad |= ((v[k][0] ^ tag) | (v[k][1] ^ tag)) | ((v[k][2] ^ tag) | (v[k][3] ^ tag));
      if (__builtin_amdgcn_ballot_w64((bad & 0xffu) != 0) == 0 || ++spins > (1 << 20)) break;
      __builtin_amdgcn_s_sleep(2);
    }
    float r[4];
#pragma unroll
    for (int i = 0; i < 4; ++i) {
      float s = asf(v[0][i] ^ tag);
#pragma unroll
      for (int k = 1; k < 8; ++k) s += asf(v[k][i] ^ tag);
      r[i] = s;
    }
    float y[4];
#pragma unroll
    for (int i = 0; i < 4; ++i) { const float t = fold32(r[i], r[i]); y[i] = fold16(t, t); }
    const u32x2 pk = {pk2(y[0], y[1]), pk2(y[2], y[3])};
    __builtin_amdgcn_raw_buffer_store_b64(pk, rout, lane < 16 ? 16 * c : OOB, oo, 0);
    if (p == 0 && lane == 0) __builtin_amdgcn_raw_buffer_store_b32(epoch, rs, 0, WS_EPOCH + bp * 256, SC1);
  }
}

extern "C" void kernel_launch(void* const* d_in, const int* in_sizes, int n_in, void* d_out, int out_size, void* d_ws, size_t ws_size, hipStream_t stream) {
  const u16* x = (const u16*)d_in[0];
  const u16* kc = (const u16*)d_in[1];
  const u16* vc = (const u16*)d_in[2];
  const u16* wq = (const u16*)d_in[3];
  const u16* wo = (const u16*)d_in[4];
  mha_fused<<<256, 512, 0, stream>>>(x, kc, vc, wq, wo, (u16*)d_out, (char*)d_ws);
}
```
